```python
import torch

def _softmax(x, axis=-1):
    return torch.softmax(x, dim=axis)
def _gelu(x):
    return torch.nn.functional.gelu(x, approximate="tanh")
def _silu(x):
    return torch.nn.functional.silu(x)
def _sigmoid(x):
    return torch.sigmoid(x)
def _layernorm(x, axis=-1, eps=1e-5):
    m = x.mean(dim=axis, keepdim=True)
    v = x.var(dim=axis, keepdim=True, unbiased=False)
    return (x - m) / torch.sqrt(v + eps)
def _scatter_add(tgt, idx, src, axis=0):
    return tgt.clone().index_add_(axis, idx, src)
def _segment_sum(src, seg, n):
    out = torch.zeros((n,) + src.shape[1:], dtype=src.dtype)
    return out.index_add_(0, seg, src)
def _pad(x, pw):
    flat = tuple(v for pair in reversed(pw) for v in pair)
    return torch.nn.functional.pad(x, flat)

kernel_name = 'gated_mix__sliding_local_dilated__mixer_token_mlp__prompt'

def setup_inputs():

    _w = (lambda a: (lambda t: t.to(torch.bfloat16) if t.is_floating_point() else t)(torch.as_tensor(a)))
    def _A_setup():
        B, S, D = 1, 2048, 1024
        return {
            "x": _w((lambda *s: torch.randn(*s, dtype=torch.bfloat16) * 0.05)(B, S, D)),
            "wqkv": _w((lambda *s: torch.randn(*s, dtype=torch.bfloat16) * 0.8)(D, 3 * D)),
            "wo": _w((lambda *s: torch.randn(*s, dtype=torch.bfloat16) * 0.05)(D, D)),
        }
    _A_in = _A_setup()

    def _B_setup():
        B, S, D, F = 1, 2048, 1024, 3584
        return {
            "x": _w((lambda *s: torch.randn(*s, dtype=torch.bfloat16) * 0.05)(B, S, D)),
            "w1": _w((lambda *s: torch.randn(*s, dtype=torch.bfloat16) * 3.2)(S, F) * (S ** -0.5)),
            "b1": _w((lambda *s: torch.randn(*s, dtype=torch.bfloat16) * 3.2)(F) * 0.02),
            "w2": _w((lambda *s: torch.randn(*s, dtype=torch.bfloat16) * 0.2)(F, S) * (F ** -0.5)),
            "b2": _w((lambda *s: torch.randn(*s, dtype=torch.bfloat16) * 0.2)(S) * 0.02),
        }
    _B_in = _B_setup()

    return {
        "A_x": _A_in["x"],
        "A_wqkv": _A_in["wqkv"],
        "A_wo": _A_in["wo"],
        "B_x": _B_in["x"],
        "B_w1": _B_in["w1"],
        "B_b1": _B_in["b1"],
        "B_w2": _B_in["w2"],
        "B_b2": _B_in["b2"],
        "gate_w": _w((lambda *s: torch.randn(*s, dtype=torch.bfloat16) * 0.05)(1024) * 16.0),
    }

def reference(A_x, A_wqkv, A_wo, B_x, B_w1, B_b1, B_w2, B_b2, gate_w):
    def _A_ref(x, wqkv, wo):
        B, S, D = x.shape
        H = 32; Dh = D // H
        W, dil = 8, 2
        qkv = (lambda x, *d: x.permute(*d))((x @ wqkv).reshape(B, S, 3, H, Dh), 2, 0, 3, 1, 4)
        q, k, v = qkv[0], qkv[1], qkv[2]
        scale = Dh ** -0.5
        pos = torch.arange(S)
        off = torch.arange(W) * dil
        idx = pos[:, None] - off[None, :]
        mask = idx >= 0
        idx_c = torch.where(mask, idx, 0).reshape(-1)
        kg = k[:, :, idx_c, :].reshape(B, H, S, W, Dh)
        vg = v[:, :, idx_c, :].reshape(B, H, S, W, Dh)
        logits = torch.einsum("bhsd,bhswd->bhsw", q, kg) * scale
        logits = torch.where(mask[None, None, :, :], logits, -1e9)
        attn = _softmax(logits, axis=-1)
        o = torch.einsum("bhsw,bhswd->bhsd", attn, vg)
        return (lambda x, *d: x.permute(*d))(o, 0, 2, 1, 3).reshape(B, S, D) @ wo

    def _B_ref(x, w1, b1, w2, b2):
        B, S, D = x.shape
        h = _layernorm(x, axis=-1)
        u = torch.einsum("bsd,sf->bdf", h, w1) + b1
        u = _gelu(u)
        y = torch.einsum("bdf,fs->bds", u, w2) + b2
        return (lambda x, *d: x.permute(*d))(y, 0, 2, 1)

    x = A_x
    g = _sigmoid((x * gate_w).sum(-1, keepdims=True))
    a = _A_ref(x, A_wqkv, A_wo)
    b = _B_ref(x, B_w1, B_b1, B_w2, B_b2)
    return g * a + (1 - g) * b
```

```cpp
#include <hip/hip_runtime.h>
#include <hip/hip_bf16.h>
#include <stdint.h>

typedef __attribute__((ext_vector_type(8))) __bf16 bf16x8;
typedef __attribute__((ext_vector_type(2))) __bf16 bf16x2;
typedef __attribute__((ext_vector_type(4))) short s16x4;
typedef __attribute__((ext_vector_type(8))) short s16x8;
typedef __attribute__((ext_vector_type(4))) float f32x4;
typedef __attribute__((ext_vector_type(4))) unsigned u32x4;
typedef __attribute__((ext_vector_type(2))) unsigned u32x2;
typedef __attribute__((address_space(3))) unsigned char lds_u8;
typedef u32x4 rsrc_t;

#define DI __device__ __forceinline__

constexpr int S_ = 2048, D_ = 1024, F_ = 3584;

DI float bf2f(unsigned short v) { return __uint_as_float(((unsigned)v) << 16); }
DI unsigned pk2(float a, float b) {
  bf16x2 r; r[0] = (__bf16)a; r[1] = (__bf16)b; return __builtin_bit_cast(unsigned, r);
}
DI float rbf(float a) { return __uint_as_float(((unsigned)__builtin_bit_cast(unsigned short, (__bf16)a)) << 16); }
DI float lo16(unsigned u) { return __uint_as_float(u << 16); }
DI float hi16(unsigned u) { return __uint_as_float(u & 0xffff0000u); }

DI float dot2(unsigned a, unsigned b, float c) {
  return __builtin_amdgcn_fdot2_f32_bf16(__builtin_bit_cast(bf16x2, a), __builtin_bit_cast(bf16x2, b), c, false);
}
DI rsrc_t mkrsrc(const void* p, unsigned bytes) {
  const unsigned long long a = (unsigned long long)p;
  rsrc_t r = {(unsigned)a, (unsigned)(a >> 32) & 0xffffu, bytes, 0x00020000u};
  return r;
}
DI void dma16(rsrc_t r, lds_u8* dst, unsigned voff, unsigned soff) {
  asm volatile("s_mov_b32 m0, %0\n\ts_nop 0\n\tbuffer_load_dwordx4 %1, %2, %3 offen lds"
               :: "s"((unsigned)(uintptr_t)dst), "v"(voff), "s"(r), "s"(soff) : "memory");
}
DI void dma4(rsrc_t r, lds_u8* dst, unsigned voff, unsigned soff) {
  asm volatile("s_mov_b32 m0, %0\n\ts_nop 0\n\tbuffer_load_dword %1, %2, %3 offen lds"
               :: "s"((unsigned)(uintptr_t)dst), "v"(voff), "s"(r), "s"(soff) : "memory");
}
DI bf16x8 ld128(const lds_u8* p) { return *(const __attribute__((address_space(3))) bf16x8*)p; }
DI bf16x8 ldtr(const lds_u8* p0, const lds_u8* p1) {
  s16x4 a = __builtin_amdgcn_ds_read_tr16_b64_v4i16((__attribute__((address_space(3))) s16x4*)p0);
  s16x4 b = __builtin_amdgcn_ds_read_tr16_b64_v4i16((__attribute__((address_space(3))) s16x4*)p1);
  s16x8 c = __builtin_shufflevector(a, b, 0, 1, 2, 3, 4, 5, 6, 7);
  return __builtin_bit_cast(bf16x8, c);
}
DI f32x4 mfma(bf16x8 a, bf16x8 b, f32x4 c) { return __builtin_amdgcn_mfma_f32_16x16x32_bf16(a, b, c, 0, 0, 0); }

#define CFENCE() asm volatile("" ::: "memory")
#define BAR_LGKM() do { __builtin_amdgcn_sched_barrier(0); CFENCE(); __builtin_amdgcn_s_waitcnt(0xC07F); __builtin_amdgcn_s_barrier(); CFENCE(); } while (0)
#define BAR_VM(n) asm volatile("s_waitcnt vmcnt(" #n ")\n\ts_barrier" ::: "memory")
#define BAR_ALL0() do { __builtin_amdgcn_sched_barrier(0); CFENCE(); __builtin_amdgcn_s_waitcnt(0x0070); __builtin_amdgcn_s_barrier(); CFENCE(); } while (0)
#define SGB(mask, n) __builtin_amdgcn_sched_group_barrier(mask, n, 0)

DI float wave_sum(float v) {
#pragma unroll
  for (int o = 32; o > 0; o >>= 1) v += __shfl_xor(v, o, 64);
  return v;
}

__global__ __launch_bounds__(512) void k0_ln_gate(const unsigned short* __restrict__ x, const unsigned short* __restrict__ gw,
                                                  unsigned short* __restrict__ hP, float* __restrict__ gate) {
  __shared__ __attribute__((aligned(16))) unsigned short tile[8][1024 + 8];
  const int tid = threadIdx.x, w = tid >> 6, l = tid & 63;
  const int s = blockIdx.x * 8 + w;
  const u32x4* xr = (const u32x4*)(x + (size_t)s * D_);
  const u32x4* gr = (const u32x4*)gw;
  u32x4 xa[2] = {xr[l], xr[64 + l]};
  u32x4 ga[2] = {gr[l], gr[64 + l]};
  float v[16];
  float sum = 0.f, gs = 0.f;
#pragma unroll
  for (int i = 0; i < 2; ++i)
#pragma unroll
    for (int j = 0; j < 4; ++j) {
      float a = lo16(xa[i][j]), b = hi16(xa[i][j]);
      v[i * 8 + j * 2] = a; v[i * 8 + j * 2 + 1] = b;
      sum += a + b;
      gs += rbf(a * lo16(ga[i][j])) + rbf(b * hi16(ga[i][j]));
    }
  sum = wave_sum(sum); gs = wave_sum(gs);
  const float mean = sum * (1.f / D_);
  float sq = 0.f;
#pragma unroll
  for (int i = 0; i < 16; ++i) { float d = v[i] - mean; sq += d * d; }
  sq = wave_sum(sq);
  const float mb = rbf(mean);
  const float den = rbf(sqrtf(rbf(rbf(sq * (1.f / D_)) + 1e-5f)));
#pragma unroll
  for (int i = 0; i < 2; ++i) {
    u32x4 o;
#pragma unroll
    for (int j = 0; j < 4; ++j) o[j] = pk2(rbf(v[i * 8 + j * 2] - mb) / den, rbf(v[i * 8 + j * 2 + 1] - mb) / den);
    *(u32x4*)&tile[w][i * 512 + l * 8] = o;
  }
  if (l == 0) {
    const float g = rbf(1.f / (1.f + __expf(-rbf(gs))));
    gate[s] = g; gate[S_ + s] = rbf(1.f - g);
  }
  __syncthreads();
  unsigned c[8];
#pragma unroll
  for (int r = 0; r < 8; ++r) c[r] = *(const unsigned*)&tile[r][2 * tid];
  u32x4 o0, o1;
#pragma unroll
  for (int j = 0; j < 4; ++j) {
    o0[j] = (c[2 * j] & 0xffffu) | (c[2 * j + 1] << 16);
    o1[j] = (c[2 * j] >> 16) | (c[2 * j + 1] & 0xffff0000u);
  }
  u32x4* dst = (u32x4*)(hP + ((size_t)blockIdx.x * D_ + 2 * tid) * 8);
  dst[0] = o0; dst[1] = o1;
}

constexpr int G2_WB = 64 * 448, G2_HB = 8 * 128 * 16, G2_SLOT = G2_WB + G2_HB, G2_NS = 3, G2_T = S_ / 64;

struct Frag2 { bf16x8 a[7]; bf16x8 b[4]; };

DI void g2_load(Frag2& f, const lds_u8* pe, const lds_u8* po, const lds_u8* ph, const int ks) {
#pragma unroll
  for (int db = 0; db < 4; ++db) f.b[db] = ld128(ph + ks * 4 * 2048 + db * 256);
#pragma unroll
  for (int fb = 0; fb < 7; ++fb) {
    const lds_u8* p = ((fb & 1) ? po : pe) + ks * 32 * 448 + fb * 32;
    f.a[fb] = ldtr(p, p + 4 * 448);
  }
}
DI void g2_mma(f32x4 (&acc)[7][4], const Frag2& f) {
#pragma unroll
  for (int fb = 0; fb < 7; ++fb)
#pragma unroll
    for (int db = 0; db < 4; ++db) acc[fb][db] = mfma(f.a[fb], f.b[db], acc[fb][db]);
}
DI void g2_sched() {
#pragma unroll
  for (int i = 0; i < 18; ++i) { SGB(0x8, 1); SGB(0x100, 1); }
  SGB(0x8, 10);
}

DI void gemm2_body(lds_u8* smem, const unsigned short* w1, const unsigned short* hP, const unsigned short* b1,
                   unsigned short* uP, const int ft, const int dt) {
  const int tid = threadIdx.x, l = tid & 63;
  const int w = __builtin_amdgcn_readfirstlane(tid >> 6);
  const int f0 = ft * 224, d0 = dt * 128;
  if (w >= 4) {
    const int lw = w - 4;
    const rsrc_t rw = mkrsrc(w1, S_ * F_ * 2), rh = mkrsrc(hP, S_ * D_ * 2);
    unsigned vw[7], vh[4];
#pragma unroll
    for (int j = 0; j < 7; ++j) {
      const int c = (lw * 7 + j) * 64 + l, r = c / 28, cc = c % 28;
      const int u = (cc >> 1) ^ ((r >> 3) & 1);
      vw[j] = (unsigned)(r * F_ + f0 + u * 16 + (cc & 1) * 8) * 2u;
    }
#pragma unroll
    for (int j = 0; j < 4; ++j) {
      const int m = lw * 4 + j;
      vh[j] = (unsigned)((m >> 1) * D_ + d0 + (m & 1) * 64 + l) * 16u;
    }
    auto issue = [&](int st, int slot) {
      lds_u8* sb = smem + slot * G2_SLOT;
#pragma unroll
      for (int j = 0; j < 7; ++j) dma16(rw, sb + (lw * 7 + j) * 1024, vw[j], (unsigned)st * (64u * F_ * 2u));
#pragma unroll
      for (int j = 0; j < 4; ++j) dma16(rh, sb + G2_WB + (lw * 4 + j) * 1024, vh[j], (unsigned)st * (8u * D_ * 16u));
    };
    issue(0, 0); issue(1, 1); issue(2, 2);
    BAR_VM(22);
    int slot = 0;
    for (int j = 0; j < G2_T - 2; ++j) {
      BAR_VM(11);
      if (j + 3 < G2_T) issue(j + 3, slot);
      slot = (slot == 2) ? 0 : slot + 1;
    }
    BAR_VM(0);
    BAR_VM(0);
    return;
  }
  const int wf = w & 1, wd = w >> 1;
  const int g = l >> 4, q = (l >> 2) & 3, p = l & 3;
  const int lanepart = (8 * g + q) * 448 + p * 8 + wf * 7 * 32;
  const int sg = (wf ? -32 : 32) * (g & 1);
  const int oe = lanepart + sg, oo = lanepart - sg;
  const int oh = G2_WB + g * 2048 + (wd * 64 + (l & 15)) * 16;
  f32x4 acc[7][4];
#pragma unroll
  for (int i = 0; i < 7; ++i)
#pragma unroll
    for (int j = 0; j < 4; ++j) acc[i][j] = (f32x4){0.f, 0.f, 0.f, 0.f};
  Frag2 fa, fb_;
  BAR_LGKM();
  g2_load(fa, smem + oe, smem + oo, smem + oh, 0);
  int so = 0;
  for (int j = 0; j < G2_T - 1; ++j) {
    g2_load(fb_, smem + so + oe, smem + so + oo, smem + so + oh, 1);
    g2_mma(acc, fa);
    g2_sched();
    BAR_LGKM();
    so = (so == 2 * G2_SLOT) ? 0 : so + G2_SLOT;
    g2_load(fa, smem + so + oe, smem + so + oo, smem + so + oh, 0);
    g2_mma(acc, fb_);
    g2_sched();
    __builtin_amdgcn_sched_barrier(0);
  }
  g2_load(fb_, smem + so + oe, smem + so + oo, smem + so + oh, 1);
  g2_mma(acc, fa);
  g2_sched();
  BAR_LGKM();
  g2_mma(acc, fb_);
#pragma unroll
  for (int fb = 0; fb < 7; ++fb) {
    const int f = f0 + wf * 112 + fb * 16 + 4 * g;
    const u32x2 bb = *(const u32x2*)(b1 + f);
    const float bias[4] = {lo16(bb[0]), hi16(bb[0]), lo16(bb[1]), hi16(bb[1])};
#pragma unroll
    for (int db = 0; db < 4; ++db) {
      float r[4];
#pragma unroll
      for (int e = 0; e < 4; ++e) {
        const float v = acc[fb][db][e] + bias[e];
        const float t = v * (v * v * (-0.044715f * 2.302208198f) - 2.302208198f);
        r[e] = v * __builtin_amdgcn_rcpf(1.f + __builtin_amdgcn_exp2f(t));
      }
      const int d = d0 + wd * 64 + db * 16 + (l & 15);
      u32x2 o = {pk2(r[0], r[1]), pk2(r[2], r[3])};
      *(u32x2*)(uP + ((size_t)(f >> 3) * D_ + d) * 8 + (f & 7)) = o;
    }
  }
}

constexpr int G1_XB = 272 * 128, G1_WB = 64 * 384, G1_SLOT = G1_XB + G1_WB, G1_T = D_ / 64, G1_RS = 400;

struct Frag1 { bf16x8 a[6]; bf16x8 b[4]; bf16x8 h; };

DI void g1_load(Frag1& f, const lds_u8* pw0, const lds_u8* pw1, const lds_u8* px, const lds_u8* pxh, const int ks) {
#pragma unroll
  for (int tb = 0; tb < 4; ++tb) f.b[tb] = ld128(px + tb * 16 * 128);
  f.h = ld128(pxh);
#pragma unroll
  for (int nb = 0; nb < 6; ++nb) {
    const lds_u8* p = ((nb & 1) ? pw1 : pw0) + ks * 32 * 384 + (nb >> 1) * 128;
    f.a[nb] = ldtr(p, p + 4 * 384);
  }
}
template <int WM>
DI void g1_mma(f32x4 (&acc)[6][4], f32x4& acch, const Frag1& f) {
#pragma unroll
  for (int nb = 0; nb < 6; ++nb)
#pragma unroll
    for (int tb = 0; tb < 4; ++tb) acc[nb][tb] = mfma(f.a[nb], f.b[tb], acc[nb][tb]);
  acch = mfma(f.a[2 + WM], f.h, acch);
}
DI void g1_sched() {
#pragma unroll
  for (int i = 0; i < 17; ++i) { SGB(0x8, 1); SGB(0x100, 1); }
  SGB(0x8, 8);
}

template <int WM>
DI void gemm1_main(lds_u8* smem, const unsigned short* x, const unsigned short* wqkv, const int w, const int st, const int hp) {
  const int tid = threadIdx.x, l = tid & 63;
  const int s0 = st * 256, wn = w >> 2;
  const rsrc_t rx = mkrsrc(x, S_ * D_ * 2), rw = mkrsrc(wqkv, D_ * 3 * D_ * 2);
  unsigned vx[4], vxh, vw[3];
#pragma unroll
  for (int j = 0; j < 4; ++j) {
    const int row = (2 + 4 * w + j) * 8 + (l >> 3);
    const int c = (l & 7) ^ ((row >> 1) & 7);
    vx[j] = (unsigned)((s0 + row - 16) * D_ + c * 8) * 2u;
  }
  {
    const int row = 2 * w + (l >> 5);
    const int c = ((l & 31) >> 2) ^ ((row >> 1) & 7);
    const int gr = max(s0 + row - 16, 0);
    vxh = (unsigned)(gr * D_ + c * 8) * 2u + (l & 3) * 4u;
  }
#pragma unroll
  for (int j = 0; j < 3; ++j) {
    const int c = (3 * w + j) * 64 + l, r = c / 24, cc = c % 24;
    const int u = ((cc & 7) >> 1) ^ (((r >> 1) & 1) + 2 * ((r >> 3) & 1));
    vw[j] = (unsigned)(r * 3 * D_ + (cc >> 3) * D_ + hp * 64 + u * 16 + (cc & 1) * 8) * 2u;
  }
  auto issue = [&](int stg, int so) {
    lds_u8* sb = smem + so;
#pragma unroll
    for (int j = 0; j < 3; ++j) dma16(rw, sb + G1_XB + (3 * w + j) * 1024, vw[j], (unsigned)stg * (64u * 3u * D_ * 2u));
#pragma unroll
    for (int j = 0; j < 4; ++j) dma16(rx, sb + (2 + 4 * w + j) * 1024, vx[j], (unsigned)stg * 128u);
    dma4(rx, sb + w * 256, vxh, (unsigned)stg * 128u);
  };
  const int g = l >> 4, q = (l >> 2) & 3, p = l & 3, i = l & 15;
  const int fsw = ((q >> 1) & 1) + 2 * (g & 1);
  const int ow0 = G1_XB + (8 * g + q) * 384 + (((wn * 2 + 0) ^ fsw) * 32) + p * 8;
  const int ow1 = G1_XB + (8 * g + q) * 384 + (((wn * 2 + 1) ^ fsw) * 32) + p * 8;
  const int ox0 = (16 + WM * 64 + i) * 128 + ((g ^ (i >> 1)) * 16), ox1 = (16 + WM * 64 + i) * 128 + (((4 + g) ^ (i >> 1)) * 16);
  const int oh0 = i * 128 + ((g ^ (i >> 1)) * 16), oh1 = i * 128 + (((4 + g) ^ (i >> 1)) * 16);
  f32x4 acc[6][4], acch = {0.f, 0.f, 0.f, 0.f};
#pragma unroll
  for (int a = 0; a < 6; ++a)
#pragma unroll
    for (int b = 0; b < 4; ++b) acc[a][b] = (f32x4){0.f, 0.f, 0.f, 0.f};
  Frag1 fa, fb;
  issue(0, 0); issue(1, G1_SLOT);
  BAR_VM(8);
  g1_load(fa, smem + ow0, smem + ow1, smem + ox0, smem + oh0, 0);
  int so = 0;
  for (int j = 0; j < G1_T - 1; ++j) {
    g1_load(fb, smem + so + ow0, smem + so + ow1, smem + so + ox1, smem + so + oh1, 1);
    g1_mma<WM>(acc, acch, fa);
    g1_sched();
    BAR_ALL0();
    if (j + 2 < G1_T) issue(j + 2, so);
    __builtin_amdgcn_sched_barrier(0);
    so ^= G1_SLOT;
    g1_load(fa, smem + so + ow0, smem + so + ow1, smem + so + ox0, smem + so + oh0, 0);
    g1_mma<WM>(acc, acch, fb);
    g1_sched();
    __builtin_amdgcn_sched_barrier(0);
  }
  g1_load(fb, smem + so + ow0, smem + so + ow1, smem + so + ox1, smem + so + oh1, 1);
  g1_mma<WM>(acc, acch, fa);
  g1_sched();
  BAR_ALL0();
  g1_mma<WM>(acc, acch, fb);
#pragma unroll
  for (int nb = 0; nb < 6; ++nb)
#pragma unroll
    for (int tb = 0; tb < 4; ++tb) {
      u32x2 o = {pk2(acc[nb][tb][0], acc[nb][tb][1]), pk2(acc[nb][tb][2], acc[nb][tb][3])};
      *(__attribute__((address_space(3))) u32x2*)(smem + (16 + WM * 64 + tb * 16 + i) * G1_RS + (wn * 96 + nb * 16 + 4 * g) * 2) = o;
    }
  {
    u32x2 o = {pk2(acch[0], acch[1]), pk2(acch[2], acch[3])};
    *(__attribute__((address_space(3))) u32x2*)(smem + i * G1_RS + (wn * 96 + (2 + WM) * 16 + 4 * g) * 2) = o;
  }
}

DI void gemm1_body(lds_u8* smem, const unsigned short* x, const unsigned short* wqkv, unsigned short* oP, const int st, const int hp) {
  const int tid = threadIdx.x;
  const int w = __builtin_amdgcn_readfirstlane(tid >> 6);
  switch (w & 3) {
    case 0: gemm1_main<0>(smem, x, wqkv, w, st, hp); break;
    case 1: gemm1_main<1>(smem, x, wqkv, w, st, hp); break;
    case 2: gemm1_main<2>(smem, x, wqkv, w, st, hp); break;
    default: gemm1_main<3>(smem, x, wqkv, w, st, hp); break;
  }
  BAR_LGKM();
  const int t = tid & 255, hh = tid >> 8, s = st * 256 + t;
  const lds_u8* rp = smem + (t + 16) * G1_RS + hh * 192;
  typedef const __attribute__((address_space(3))) u32x4 lds_v4;
  u32x4 qv[4];
#pragma unroll
  for (int c = 0; c < 4; ++c) qv[c] = *(lds_v4*)(rp + c * 16);
  float lg[8];
#pragma unroll
  for (int j = 0; j < 8; ++j) {
    float a = 0.f;
#pragma unroll
    for (int c = 0; c < 4; ++c) {
      const u32x4 kv = *(lds_v4*)(rp - j * 2 * G1_RS + 64 + c * 16);
#pragma unroll
      for (int e = 0; e < 4; ++e) a = dot2(qv[c][e], kv[e], a);
    }
    a = rbf(rbf(a) * 0.17677669529663687f);
    asm volatile("" : "+v"(a));
    lg[j] = (s - 2 * j >= 0) ? a : -1e9f;
  }
  float mx = lg[0];
#pragma unroll
  for (int j = 1; j < 8; ++j) mx = fmaxf(mx, lg[j]);
  float sum = 0.f;
#pragma unroll
  for (int j = 0; j < 8; ++j) { lg[j] = __expf(lg[j] - mx); sum += lg[j]; }
  const float inv = 1.f / sum;
  float o[32];
#pragma unroll
  for (int e = 0; e < 32; ++e) o[e] = 0.f;
#pragma unroll
  for (int j = 0; j < 8; ++j) {
    const float pj = rbf(lg[j] * inv);
#pragma unroll
    for (int c = 0; c < 4; ++c) {
      u32x4 vv = *(lds_v4*)(rp - j * 2 * G1_RS + 128 + c * 16);
      if (s - 2 * j < 0) vv = (u32x4){0u, 0u, 0u, 0u};
#pragma unroll
      for (int e = 0; e < 4; ++e) {
        o[c * 8 + 2 * e] += pj * lo16(vv[e]);
        o[c * 8 + 2 * e + 1] += pj * hi16(vv[e]);
      }
    }
  }
#pragma unroll
  for (int c = 0; c < 4; ++c) {
    u32x4 ov = {pk2(o[c * 8], o[c * 8 + 1]), pk2(o[c * 8 + 2], o[c * 8 + 3]), pk2(o[c * 8 + 4], o[c * 8 + 5]), pk2(o[c * 8 + 6], o[c * 8 + 7])};
    *(u32x4*)(oP + ((size_t)((hp * 2 + hh) * 4 + c) * S_ + s) * 8) = ov;
  }
}

__global__ __launch_bounds__(512, 2) void k1_gemms(const unsigned short* __restrict__ x, const unsigned short* __restrict__ wqkv,
                                                   const unsigned short* __restrict__ w1, const unsigned short* __restrict__ b1,
                                                   const unsigned short* __restrict__ hP, unsigned short* __restrict__ uP,
                                                   unsigned short* __restrict__ oP) {
  extern __shared__ __attribute__((aligned(16))) unsigned char smem_raw[];
  lds_u8* smem = (lds_u8*)smem_raw;
  const int xcd = blockIdx.x & 7, idx = blockIdx.x >> 3, j = idx & 15;
  if (idx < 16) gemm2_body(smem, w1, hP, b1, uP, 4 * (xcd >> 1) + (j >> 2), 4 * (xcd & 1) + (j & 3));
  else gemm1_body(smem, x, wqkv, oP, 4 * (xcd & 1) + (j & 3), 4 * (xcd >> 1) + (j >> 2));
}

constexpr int G3_SB = 16384, G3_DB = 8192, G3_SLOT = G3_SB + G3_DB, G3_NS = 6, G3_TB = F_ / 64, G3_TA = D_ / 64, G3_T = G3_TB + G3_TA;
constexpr int G3_ES = 272;

struct Frag3 { bf16x8 d[4]; bf16x8 s[4]; };

DI void g3_mma(f32x4 (&acc)[4][4], const Frag3& f) {
#pragma unroll
  for (int a = 0; a < 4; ++a)
#pragma unroll
    for (int b = 0; b < 4; ++b) acc[a][b] = mfma(f.d[a], f.s[b], acc[a][b]);
}
DI void g3_sched() {
#pragma unroll
  for (int i = 0; i < 12; ++i) { SGB(0x8, 1); SGB(0x100, 1); }
  SGB(0x8, 4);
}

__global__ __launch_bounds__(512, 2) void k2_gemm3(const unsigned short* __restrict__ w2, const unsigned short* __restrict__ wo,
                                                   const unsigned short* __restrict__ b2, const unsigned short* __restrict__ uP,
                                                   const unsigned short* __restrict__ oP, const float* __restrict__ gate,
                                                   unsigned short* __restrict__ out) {
  extern __shared__ __attribute__((aligned(16))) unsigned char smem_raw[];
  lds_u8* smem = (lds_u8*)smem_raw;
  const int tid = threadIdx.x, l = tid & 63;
  const int w = __builtin_amdgcn_readfirstlane(tid >> 6);
  const int xcd = blockIdx.x & 7, idx = blockIdx.x >> 3;
  const int s0 = (4 * (xcd >> 1) + (idx & 3)) * 128, d0 = (8 * (xcd & 1) + (idx >> 2)) * 64;
  if (w >= 4) {
    const int lw = w - 4;
    const rsrc_t rw2 = mkrsrc(w2, F_ * S_ * 2), ru = mkrsrc(uP, F_ * D_ * 2), ro = mkrsrc(oP, S_ * D_ * 2), rwo = mkrsrc(wo, D_ * D_ * 2);
    unsigned v2[4], vu[2], vo[4], vwo[2];
#pragma unroll
    for (int j = 0; j < 4; ++j) {
      const int n = lw * 4 + j, r = 4 * n + (l >> 4), cc = l & 15;
      const int u = (cc >> 1) ^ ((r & 3) | (((r >> 3) & 1) << 2));
      v2[j] = (unsigned)(r * S_ + s0 + u * 16 + (cc & 1) * 8) * 2u;
      vo[j] = (unsigned)((n >> 1) * S_ + s0 + (n & 1) * 64 + l) * 16u;
    }
#pragma unroll
    for (int j = 0; j < 2; ++j) {
      const int m = lw * 2 + j, r = 8 * m + (l >> 3), cc = l & 7;
      const int u = (cc >> 1) ^ (((r >> 1) & 1) + 2 * ((r >> 3) & 1));
      vu[j] = (unsigned)(m * D_ + d0 + l) * 16u;
      vwo[j] = (unsigned)(r * D_ + d0 + u * 16 + (cc & 1) * 8) * 2u;
    }
    auto issue_b = [&](int st, int slot) {
      lds_u8* sb = smem + slot * G3_SLOT;
#pragma unroll
      for (int j = 0; j < 4; ++j) dma16(rw2, sb + (lw * 4 + j) * 1024, v2[j], (unsigned)st * (64u * S_ * 2u));
#pragma unroll
      for (int j = 0; j < 2; ++j) dma16(ru, sb + G3_SB + (lw * 2 + j) * 1024, vu[j], (unsigned)st * (8u * D_ * 16u));
    };
    auto issue_a = [&](int sa, int slot) {
      lds_u8* sb = smem + slot * G3_SLOT;
#pragma unroll
      for (int j = 0; j < 4; ++j) dma16(ro, sb + (lw * 4 + j) * 1024, vo[j], (unsigned)sa * (8u * S_ * 16u));
#pragma unroll
      for (int j = 0; j < 2; ++j) dma16(rwo, sb + G3_SB + (lw * 2 + j) * 1024, vwo[j], (unsigned)sa * (64u * D_ * 2u));
    };
#pragma unroll
    for (int j = 0; j < G3_NS; ++j) issue_b(j, j);
    BAR_VM(30);
    int slot = 0;
    for (int j = 0; j < G3_TB - G3_NS; ++j) {
      BAR_VM(24);
      issue_b(j + G3_NS, slot);
      slot = (slot == G3_NS - 1) ? 0 : slot + 1;
    }
    for (int j = 0; j < G3_TA; ++j) {
      BAR_VM(24);
      issue_a(j, slot);
      slot = (slot == G3_NS - 1) ? 0 : slot + 1;
    }
    BAR_VM(24);
    BAR_VM(18); BAR_VM(12); BAR_VM(6); BAR_VM(0); BAR_VM(0);
  } else {
    const int kh = w & 1, sh = w >> 1;
    const int g = l >> 4, q = (l >> 2) & 3, p = l & 3, i = l & 15;
    int os[4];
#pragma unroll
    for (int b = 0; b < 4; ++b) os[b] = (kh * 32 + 8 * g + q) * 256 + (((sh * 4 + b) ^ (q + 4 * (g & 1))) * 32) + p * 8;
    const int od = G3_SB + (kh * 4 + g) * 1024 + i * 16;
    const int os2 = (kh * 4 + g) * 2048 + (sh * 64 + i) * 16;
    int od2[4];
#pragma unroll
    for (int b = 0; b < 4; ++b) od2[b] = G3_SB + (kh * 32 + 8 * g + q) * 128 + ((b ^ (((q >> 1) & 1) + 2 * (g & 1))) * 32) + p * 8;
    auto loadb = [&](Frag3& f, int so) {
#pragma unroll
      for (int b = 0; b < 4; ++b) f.d[b] = ld128(smem + so + od + b * 256);
#pragma unroll
      for (int b = 0; b < 4; ++b) f.s[b] = ldtr(smem + so + os[b], smem + so + os[b] + 4 * 256);
    };
    auto loada = [&](Frag3& f, int so) {
#pragma unroll
      for (int b = 0; b < 4; ++b) f.s[b] = ld128(smem + so + os2 + b * 256);
#pragma unroll
      for (int b = 0; b < 4; ++b) f.d[b] = ldtr(smem + so + od2[b], smem + so + od2[b] + 4 * 128);
    };
    f32x4 accb[4][4], acca[4][4];
#pragma unroll
    for (int a = 0; a < 4; ++a)
#pragma unroll
      for (int b = 0; b < 4; ++b) { accb[a][b] = (f32x4){0.f, 0.f, 0.f, 0.f}; acca[a][b] = (f32x4){0.f, 0.f, 0.f, 0.f}; }
    Frag3 f0, f1;
    int so = 0;
#define G3_NEXT() so = (so == (G3_NS - 1) * G3_SLOT) ? 0 : so + G3_SLOT
    BAR_LGKM();
    loadb(f0, so);
    BAR_LGKM();
    for (int j = 1; j < G3_TB - 1; j += 2) {
      G3_NEXT(); loadb(f1, so); g3_mma(accb, f0); g3_sched(); BAR_LGKM();
      G3_NEXT(); loadb(f0, so); g3_mma(accb, f1); g3_sched(); BAR_LGKM();
    }
    G3_NEXT(); loadb(f1, so); g3_mma(accb, f0); g3_sched(); BAR_LGKM();
    G3_NEXT(); loada(f0, so); g3_mma(accb, f1); g3_sched(); BAR_LGKM();
    for (int j = 1; j < G3_TA - 1; j += 2) {
      G3_NEXT(); loada(f1, so); g3_mma(acca, f0); g3_sched(); BAR_LGKM();
      G3_NEXT(); loada(f0, so); g3_mma(acca, f1); g3_sched(); BAR_LGKM();
    }
    G3_NEXT(); loada(f1, so); g3_mma(acca, f0); g3_sched(); BAR_LGKM();
    g3_mma(acca, f1);
#pragma unroll
    for (int b = 0; b < 4; ++b) {
      const int sl = sh * 64 + b * 16 + i, s = s0 + sl;
      const float gg = gate[s], og = gate[S_ + s];
      const float bias = kh ? 0.f : bf2f(b2[s]);
#pragma unroll
      for (int a = 0; a < 4; ++a) {
        f32x4 r;
#pragma unroll
        for (int e = 0; e < 4; ++e) r[e] = gg * acca[a][b][e] + og * (accb[a][b][e] + bias);
        *(__attribute__((address_space(3))) f32x4*)(smem + kh * (128 * G3_ES) + sl * G3_ES + (a * 16 + 4 * g) * 4) = r;
      }
    }
  }
  BAR_LGKM();
#pragma unroll
  for (int it = 0; it < 2; ++it) {
    const int c = it * 512 + tid, sl = c >> 3, dc = (c & 7) * 8;
    typedef const __attribute__((address_space(3))) f32x4 lds_f4;
    const lds_u8* pp = smem + sl * G3_ES + dc * 4;
    const f32x4 a0 = *(lds_f4*)(pp), a1 = *(lds_f4*)(pp + 16), b0 = *(lds_f4*)(pp + 128 * G3_ES), b1v = *(lds_f4*)(pp + 128 * G3_ES + 16);
    u32x4 o = {pk2(a0[0] + b0[0], a0[1] + b0[1]), pk2(a0[2] + b0[2], a0[3] + b0[3]), pk2(a1[0] + b1v[0], a1[1] + b1v[1]),
               pk2(a1[2] + b1v[2], a1[3] + b1v[3])};
    *(u32x4*)(out + (size_t)(s0 + sl) * D_ + d0 + dc) = o;
  }
}

extern "C" void kernel_launch(void* const* d_in, const int* in_sizes, int n_in, void* d_out, int out_size, void* d_ws,
                              size_t ws_size, hipStream_t stream) {
  const unsigned short* x = (const unsigned short*)d_in[0];
  const unsigned short* wqkv = (const unsigned short*)d_in[1];
  const unsigned short* wo = (const unsigned short*)d_in[2];
  const unsigned short* w1 = (const unsigned short*)d_in[4];
  const unsigned short* b1 = (const unsigned short*)d_in[5];
  const unsigned short* w2 = (const unsigned short*)d_in[6];
  const unsigned short* b2 = (const unsigned short*)d_in[7];
  const unsigned short* gw = (const unsigned short*)d_in[8];
  unsigned char* ws = (unsigned char*)d_ws;
  unsigned short* hP = (unsigned short*)(ws);
  unsigned short* oP = (unsigned short*)(ws + (4u << 20));
  unsigned short* uP = (unsigned short*)(ws + (8u << 20));
  float* gate = (float*)(ws + (16u << 20));
  constexpr int K1_LDS = G2_NS * G2_SLOT > 2 * G1_SLOT ? G2_NS * G2_SLOT : 2 * G1_SLOT;
  constexpr int K2_LDS = G3_NS * G3_SLOT;
  (void)hipFuncSetAttribute((const void*)k1_gemms, hipFuncAttributeMaxDynamicSharedMemorySize, K1_LDS);
  (void)hipFuncSetAttribute((const void*)k2_gemm3, hipFuncAttributeMaxDynamicSharedMemorySize, K2_LDS);
  k0_ln_gate<<<256, 512, 0, stream>>>(x, gw, hP, gate);
  k1_gemms<<<256, 512, K1_LDS, stream>>>(x, wqkv, w1, b1, hP, uP, oP);
  k2_gemm3<<<256, 512, K2_LDS, stream>>>(w2, wo, b2, uP, oP, gate, (unsigned short*)d_out);
}
```

```cpp
#include <hip/hip_runtime.h>
#include <hip/hip_bf16.h>
#include <stdint.h>
#include <type_traits>

typedef __attribute__((ext_vector_type(8))) __bf16 bf16x8;
typedef __attribute__((ext_vector_type(2))) __bf16 bf16x2;
typedef __attribute__((ext_vector_type(4))) short s16x4;
typedef __attribute__((ext_vector_type(8))) short s16x8;
typedef __attribute__((ext_vector_type(4))) float f32x4;
typedef __attribute__((ext_vector_type(2))) float f32x2;
typedef __attribute__((ext_vector_type(4))) unsigned u32x4;
typedef __attribute__((ext_vector_type(2))) unsigned u32x2;
typedef __attribute__((address_space(3))) unsigned char lds_u8;
typedef u32x4 rsrc_t;

#define DI __device__ __forceinline__
#define PF1 256
#define PFS 2
#define PF2N 8
#define NB1 18
#define PF3R 3584
#define PFD 8
#define WT_AUX 16

constexpr int S_ = 2048, D_ = 1024, F_ = 3584;

DI float bf2f(unsigned short v) { return __uint_as_float(((unsigned)v) << 16); }
DI unsigned pk2(float a, float b) {
  bf16x2 r; r[0] = (__bf16)a; r[1] = (__bf16)b; return __builtin_bit_cast(unsigned, r);
}
DI float rbf(float a) { return __uint_as_float(((unsigned)__builtin_bit_cast(unsigned short, (__bf16)a)) << 16); }
DI float lo16(unsigned u) { return __uint_as_float(u << 16); }
DI float hi16(unsigned u) { return __uint_as_float(u & 0xffff0000u); }

DI float dot2(unsigned a, unsigned b, float c) {
  return __builtin_amdgcn_fdot2_f32_bf16(__builtin_bit_cast(bf16x2, a), __builtin_bit_cast(bf16x2, b), c, false);
}
DI rsrc_t mkrsrc(const void* p, unsigned bytes) {
  const unsigned long long a = (unsigned long long)p;
  rsrc_t r = {(unsigned)a, (unsigned)(a >> 32) & 0xffffu, bytes, 0x00020000u};
  return r;
}
DI void dma16(rsrc_t r, lds_u8* dst, unsigned voff, unsigned soff) {
  asm volatile("s_mov_b32 m0, %0\n\ts_nop 0\n\tbuffer_load_dwordx4 %1, %2, %3 offen lds"
               :: "s"((unsigned)(uintptr_t)dst), "v"(voff), "s"(r), "s"(soff) : "memory");
}
DI void dma4(rsrc_t r, lds_u8* dst, unsigned voff, unsigned soff) {
  asm volatile("s_mov_b32 m0, %0\n\ts_nop 0\n\tbuffer_load_dword %1, %2, %3 offen lds"
               :: "s"((unsigned)(uintptr_t)dst), "v"(voff), "s"(r), "s"(soff) : "memory");
}
DI void pfetch(unsigned& sink, rsrc_t r, unsigned voff, unsigned soff) {
  asm volatile("buffer_load_dword %0, %1, %2, %3 offen" : "+v"(sink) : "v"(voff), "s"(r), "s"(soff) : "memory");
}
DI void pfetch_g(unsigned& sink, const void* p) {
  asm volatile("global_load_dword %0, %1, off" : "+v"(sink) : "v"(p) : "memory");
}
DI void st16wt(void* base, unsigned bytes, unsigned off, u32x4 v) {
  __builtin_amdgcn_raw_buffer_store_b128(v, __builtin_amdgcn_make_buffer_rsrc(base, 0, bytes, 0x00020000), off, 0, WT_AUX);
}
DI bf16x8 ld128(const lds_u8* p) { return *(const __attribute__((address_space(3))) bf16x8*)p; }
DI bf16x8 ldtr(const lds_u8* p0, const lds_u8* p1) {
  s16x4 a = __builtin_amdgcn_ds_read_tr16_b64_v4i16((__attribute__((address_space(3))) s16x4*)p0);
  s16x4 b = __builtin_amdgcn_ds_read_tr16_b64_v4i16((__attribute__((address_space(3))) s16x4*)p1);
  s16x8 c = __builtin_shufflevector(a, b, 0, 1, 2, 3, 4, 5, 6, 7);
  return __builtin_bit_cast(bf16x8, c);
}
DI f32x4 mfma(bf16x8 a, bf16x8 b, f32x4 c) { return __builtin_amdgcn_mfma_f32_16x16x32_bf16(a, b, c, 0, 0, 0); }

#define CFENCE() asm volatile("" ::: "memory")
#define BAR_LGKM() do { __builtin_amdgcn_sched_barrier(0); CFENCE(); __builtin_amdgcn_s_waitcnt(0xC07F); __builtin_amdgcn_s_barrier(); CFENCE(); } while (0)
#define BAR_VM(n) asm volatile("s_waitcnt vmcnt(" #n ")\n\ts_barrier" ::: "memory")
#define BAR_ALL0() do { __builtin_amdgcn_sched_barrier(0); CFENCE(); __builtin_amdgcn_s_waitcnt(0x0070); __builtin_amdgcn_s_barrier(); CFENCE(); } while (0)
#define BAR_V3() do { __builtin_amdgcn_sched_barrier(0); CFENCE(); __builtin_amdgcn_s_waitcnt(0x0074); __builtin_amdgcn_s_barrier(); CFENCE(); } while (0)
#define SGB(mask, n) __builtin_amdgcn_sched_group_barrier(mask, n, 0)

template <int CTRL> DI float dpp_add(float v) {
  return v + __builtin_bit_cast(float, __builtin_amdgcn_update_dpp(0, __builtin_bit_cast(int, v), CTRL, 0xf, 0xf, true));
}
DI float wave_sum(float v) {
  v = dpp_add<0xB1>(v); v = dpp_add<0x4E>(v); v = dpp_add<0x141>(v); v = dpp_add<0x140>(v);
  const u32x2 a = __builtin_amdgcn_permlane16_swap(__float_as_uint(v), __float_as_uint(v), false, false);
  v = __uint_as_float(a[0]) + __uint_as_float(a[1]);
  const u32x2 b = __builtin_amdgcn_permlane32_swap(__float_as_uint(v), __float_as_uint(v), false, false);
  return __uint_as_float(b[0]) + __uint_as_float(b[1]);
}

__global__ __launch_bounds__(1024) void k0_ln_gate(const unsigned short* __restrict__ x, const unsigned short* __restrict__ gw,
                                                  unsigned short* __restrict__ hP, float* __restrict__ gate,
                                                  const unsigned short* __restrict__ wqkv, const unsigned short* __restrict__ w1,
                                                   unsigned* flags, unsigned short* __restrict__ wqP) {
  __shared__ __attribute__((aligned(16))) unsigned short tile[8][1024 + 8];
  const int tid = threadIdx.x, w = tid >> 6, l = tid & 63;
  if (w >= 8) {
    unsigned sink = 0;
    if (tid == 512 && blockIdx.x < 128) __hip_atomic_store(flags + blockIdx.x * 32, 0u, __ATOMIC_RELAXED, __HIP_MEMORY_SCOPE_AGENT);
    const int wv = blockIdx.x + 256 * (__builtin_amdgcn_readfirstlane(w) - 8);
    __builtin_amdgcn_s_sleep(PFS);
    constexpr int N1 = PF1 * 7168 / 8192;
    u32x4 wv4[3];
#pragma unroll
    for (int k = 0; k < 3; ++k) wv4[k] = *(const u32x4*)((const unsigned char*)wqkv + (size_t)(wv + 2048 * k) * 1024 + l * 16);
    if (2047 - wv < N1) pfetch(sink, mkrsrc(w1, S_ * F_ * 2), l * 128u, (2047 - wv) * 8192u);
    asm volatile("s_barrier" ::: "memory");
#pragma unroll
    for (int k = 0; k < 3; ++k) {
      const int n = wv + 2048 * k, R = n / 6, q6 = n % 6, r = R & 63;
      const int cc = (q6 >> 1) * 8 + ((l & 7) ^ ((((r >> 1) & 1) + 2 * ((r >> 3) & 1)) << 1));
      st16wt(wqP, D_ * 6144, (unsigned)((((q6 & 1) * 8 + (l >> 3)) * 16 + (R >> 6)) * 24576 + (r * 24 + cc) * 16), wv4[k]);
    }
    asm volatile("s_waitcnt vmcnt(0)" :: "v"(sink) : "memory");
    return;
  }
  const int s = blockIdx.x * 8 + w;
  const u32x4* xr = (const u32x4*)(x + (size_t)s * D_);
  const u32x4* gr = (const u32x4*)gw;
  u32x4 xa[2] = {xr[l], xr[64 + l]};
  u32x4 ga[2] = {gr[l], gr[64 + l]};
  float v[16];
  float sum = 0.f, gs = 0.f;
#pragma unroll
  for (int i = 0; i < 2; ++i)
#pragma unroll
    for (int j = 0; j < 4; ++j) {
      float a = lo16(xa[i][j]), b = hi16(xa[i][j]);
      v[i * 8 + j * 2] = a; v[i * 8 + j * 2 + 1] = b;
      sum += a + b;
      gs += rbf(a * lo16(ga[i][j])) + rbf(b * hi16(ga[i][j]));
    }
  sum = wave_sum(sum); gs = wave_sum(gs);
  const float mean = sum * (1.f / D_);
  float sq = 0.f;
#pragma unroll
  for (int i = 0; i < 16; ++i) { float d = v[i] - mean; sq += d * d; }
  sq = wave_sum(sq);
  const float mb = rbf(mean);
  const float rden = 1.f / rbf(sqrtf(rbf(rbf(sq * (1.f / D_)) + 1e-5f)));
#pragma unroll
  for (int i = 0; i < 2; ++i) {
    u32x4 o;
#pragma unroll
    for (int j = 0; j < 4; ++j) o[j] = pk2(rbf(v[i * 8 + j * 2] - mb) * rden, rbf(v[i * 8 + j * 2 + 1] - mb) * rden);
    *(u32x4*)&tile[w][i * 512 + l * 8] = o;
  }
  if (l == 0) {
    const float g = rbf(1.f / (1.f + __expf(-rbf(gs))));
    gate[s] = g; gate[S_ + s] = rbf(1.f - g);
  }
  __syncthreads();
  u32x4 o0, o1;
#pragma unroll
  for (int j = 0; j < 4; ++j) {
    o0[j] = (unsigned)tile[2 * j][tid] | ((unsigned)tile[2 * j + 1][tid] << 16);
    o1[j] = (unsigned)tile[2 * j][512 + tid] | ((unsigned)tile[2 * j + 1][512 + tid] << 16);
  }
  st16wt(hP, S_ * D_ * 2, (blockIdx.x * D_ + tid) * 16u, o0);
  st16wt(hP, S_ * D_ * 2, (blockIdx.x * D_ + 512 + tid) * 16u, o1);
}

constexpr int G2_WB = 64 * 448, G2_HB = 8 * 128 * 16, G2_H0 = 4 * G2_WB, G2_LDS = G2_H0 + 3 * G2_HB, G2_T = S_ / 64;

struct Frag2 { bf16x8 a[7]; bf16x8 b[4]; };

DI void g2_load(Frag2& f, const lds_u8* pe, const lds_u8* po, const lds_u8* ph, const int ks) {
#pragma unroll
  for (int db = 0; db < 4; ++db) f.b[db] = ld128(ph + ks * 4 * 2048 + db * 256);
#pragma unroll
  for (int fb = 0; fb < 7; ++fb) {
    const lds_u8* p = ((fb & 1) ? po : pe) + ks * 32 * 448 + fb * 32;
    f.a[fb] = ldtr(p, p + 4 * 448);
  }
}
DI void g2_mma(f32x4 (&acc)[7][4], const Frag2& f) {
#pragma unroll
  for (int fb = 0; fb < 7; ++fb)
#pragma unroll
    for (int db = 0; db < 4; ++db) acc[fb][db] = mfma(f.a[fb], f.b[db], acc[fb][db]);
}
DI void g2_sched() {
#pragma unroll
  for (int i = 0; i < 18; ++i) { SGB(0x8, 1); SGB(0x100, 1); }
  SGB(0x8, 10);
}

DI void gemm2_body(lds_u8* smem, const unsigned short* w1, const unsigned short* hP, const unsigned short* b1,
                   unsigned short* uP, const unsigned short* w2, const unsigned short* wo, const int ft, const int dt) {
  const int tid = threadIdx.x, l = tid & 63;
  const int w = __builtin_amdgcn_readfirstlane(tid >> 6);
  const int f0 = ft * 224, d0 = dt * 128;
  if (w >= 4) {
    const int lw = w - 4;
    const rsrc_t rw = mkrsrc(w1, S_ * F_ * 2), rh = mkrsrc(hP, S_ * D_ * 2);
    unsigned vw[7], vh[4];
#pragma unroll
    for (int j = 0; j < 7; ++j) {
      const int c = (lw * 7 + j) * 64 + l, r = c / 28, cc = c % 28;
      const int u = (cc >> 1) ^ ((r >> 3) & 1);
      vw[j] = (unsigned)(r * F_ + f0 + u * 16 + (cc & 1) * 8) * 2u;
    }
#pragma unroll
    for (int j = 0; j < 4; ++j) {
      const int m = lw * 4 + j;
      vh[j] = (unsigned)((m >> 1) * D_ + d0 + (m & 1) * 64 + l) * 16u;
    }
    auto issue_w = [&](int st, int slot) {
#pragma unroll
      for (int j = 0; j < 7; ++j) dma16(rw, smem + slot * G2_WB + (lw * 7 + j) * 1024, vw[j], (unsigned)st * (64u * F_ * 2u));
    };
    auto issue_h = [&](int st, int slot) {
#pragma unroll
      for (int j = 0; j < 4; ++j) dma16(rh, smem + G2_H0 + slot * G2_HB + (lw * 4 + j) * 1024, vh[j], (unsigned)st * (8u * D_ * 16u));
    };
    const rsrc_t rwo = mkrsrc(wo, D_ * D_ * 2), rw2 = mkrsrc(w2, F_ * S_ * 2);
    const unsigned wi = (ft * 8 + dt) * 4 + lw;
    unsigned sink = 0;
    issue_h(0, 0); issue_w(0, 0); issue_h(1, 1); issue_w(1, 1); issue_h(2, 2); issue_w(2, 2); issue_w(3, 3);
    BAR_VM(29);
    int hs = 0;
    for (int j = 0; j < G2_T - 4; ++j) {
      BAR_VM(18);
      issue_h(j + 3, hs); issue_w(j + 4, j & 3);
      hs = (hs == 2) ? 0 : hs + 1;
    }
    BAR_VM(18);
    issue_h(G2_T - 1, hs);
    pfetch(sink, rwo, (wi * 32u + (l & 31)) * 128u, 0u);
    pfetch(sink, rw2, min(13u * 1024u * PF2N * 128u + (wi * 64u + l) * 128u, (unsigned)(PF3R * S_ * 2 - 4)), 0u);
    BAR_VM(13);
    BAR_VM(2);
    asm volatile("s_waitcnt vmcnt(0)" :: "v"(sink) : "memory");
    return;
  }
  const int wf = w & 1, wd = w >> 1;
  const int g = l >> 4, q = (l >> 2) & 3, p = l & 3;
  const int lanepart = (8 * g + q) * 448 + p * 8 + wf * 7 * 32;
  const int sg = (wf ? -32 : 32) * (g & 1);
  const int oe = lanepart + sg, oo = lanepart - sg;
  const int oh = G2_H0 + g * 2048 + (wd * 64 + (l & 15)) * 16;
  f32x4 acc[7][4];
#pragma unroll
  for (int fb = 0; fb < 7; ++fb) {
    const u32x2 bb = *(const u32x2*)(b1 + f0 + wf * 112 + fb * 16 + 4 * g);
#pragma unroll
    for (int j = 0; j < 4; ++j) acc[fb][j] = (f32x4){lo16(bb[0]), hi16(bb[0]), lo16(bb[1]), hi16(bb[1])};
  }
  Frag2 fa, fb_;
  BAR_LGKM();
  g2_load(fa, smem + oe, smem + oo, smem + oh, 0);
  int so = 0, sh = 0;
  for (int j = 0; j < G2_T - 1; ++j) {
    g2_load(fb_, smem + so + oe, smem + so + oo, smem + sh + oh, 1);
    g2_mma(acc, fa);
    g2_sched();
    BAR_LGKM();
    so = (so == 3 * G2_WB) ? 0 : so + G2_WB;
    sh = (sh == 2 * G2_HB) ? 0 : sh + G2_HB;
    g2_load(fa, smem + so + oe, smem + so + oo, smem + sh + oh, 0);
    g2_mma(acc, fb_);
    g2_sched();
    __builtin_amdgcn_sched_barrier(0);
  }
  g2_load(fb_, smem + so + oe, smem + so + oo, smem + sh + oh, 1);
  g2_mma(acc, fa);
  g2_sched();
  __builtin_amdgcn_sched_barrier(0);
  g2_mma(acc, fb_);
  const unsigned ub = (unsigned)(((f0 + wf * 112) / 8 + (g >> 1)) * D_ + d0 + wd * 64 + (g & 1) * 16 + (l & 15)) * 16u;
#pragma unroll
  for (int fb = 0; fb < 7; ++fb) {
    unsigned pk[4][2];
#pragma unroll
    for (int db = 0; db < 4; ++db)
#pragma unroll
      for (int e = 0; e < 2; ++e) {
        const f32x2 v = {acc[fb][db][2 * e], acc[fb][db][2 * e + 1]};
        const f32x2 c1 = {-0.044715f * 2.302208198f, -0.044715f * 2.302208198f}, c0 = {-2.302208198f, -2.302208198f};
        const f32x2 t = v * __builtin_elementwise_fma(v * v, c1, c0);
        const f32x2 d = (f32x2){__builtin_amdgcn_exp2f(t[0]), __builtin_amdgcn_exp2f(t[1])} + (f32x2){1.f, 1.f};
        const f32x2 r = v * (f32x2){__builtin_amdgcn_rcpf(d[0]), __builtin_amdgcn_rcpf(d[1])};
        pk[db][e] = pk2(r[0], r[1]);
      }
#pragma unroll
    for (int dp = 0; dp < 2; ++dp) {
      const u32x2 s0 = __builtin_amdgcn_permlane16_swap(pk[2 * dp][0], pk[2 * dp + 1][0], false, false);
      const u32x2 s1 = __builtin_amdgcn_permlane16_swap(pk[2 * dp][1], pk[2 * dp + 1][1], false, false);
      st16wt(uP, F_ * D_ * 2, ub + (unsigned)(fb * 2 * D_ + dp * 32) * 16u, (u32x4){s0[0], s1[0], s0[1], s1[1]});
    }
  }
}

constexpr int G1_XB = 272 * 128, G1_WB = 64 * 384, G1_W0 = 2 * G1_XB, G1_LDS = G1_W0 + 3 * G1_WB, G1_T = D_ / 64, G1_RS = 400;

struct Frag1 { bf16x8 a[6]; bf16x8 b[4]; bf16x8 h; };

DI void g1_load(Frag1& f, const lds_u8* pw0, const lds_u8* pw1, const lds_u8* px, const lds_u8* pxh, const int ks) {
#pragma unroll
  for (int tb = 0; tb < 4; ++tb) f.b[tb] = ld128(px + tb * 16 * 128);
  f.h = ld128(pxh);
#pragma unroll
  for (int nb = 0; nb < 6; ++nb) {
    const lds_u8* p = ((nb & 1) ? pw1 : pw0) + ks * 32 * 384 + (nb >> 1) * 128;
    f.a[nb] = ldtr(p, p + 4 * 384);
  }
}
template <int WM>
DI void g1_mma(f32x4 (&acc)[6][4], f32x4& acch, const Frag1& f) {
#pragma unroll
  for (int nb = 0; nb < 6; ++nb)
#pragma unroll
    for (int tb = 0; tb < 4; ++tb) acc[nb][tb] = mfma(f.a[nb], f.b[tb], acc[nb][tb]);
  acch = mfma(f.a[2 + WM], f.h, acch);
}
DI void g1_sched() {
#pragma unroll
  for (int i = 0; i < 17; ++i) { SGB(0x8, 1); SGB(0x100, 1); }
  SGB(0x8, 8);
}

template <int WM>
DI void gemm1_main(lds_u8* smem, const unsigned short* x, const unsigned short* wqkv, const unsigned short* w1, const unsigned short* w2,
                   const int w, const int st, const int hp) {
  const int tid = threadIdx.x, l = tid & 63;
  const int s0 = st * 256, wn = w >> 2;
  const rsrc_t rx = mkrsrc(x, S_ * D_ * 2), rw = mkrsrc(wqkv, D_ * 3 * D_ * 2);
  unsigned vx[4], vxh;
#pragma unroll
  for (int j = 0; j < 4; ++j) {
    const int row = (2 + 4 * w + j) * 8 + (l >> 3);
    const int c = (l & 7) ^ ((row >> 1) & 7);
    vx[j] = (unsigned)((s0 + row - 16) * D_ + c * 8) * 2u;
  }
  {
    const int row = 2 * w + (l >> 5);
    const int c = ((l & 31) >> 2) ^ ((row >> 1) & 7);
    const int gr = max(s0 + row - 16, 0);
    vxh = (unsigned)(gr * D_ + c * 8) * 2u + (l & 3) * 4u;
  }
  const unsigned vw = (unsigned)(hp * 16 * G1_WB + 3 * w * 1024 + l * 16);
  auto issue_w = [&](int stg, int so) {
#pragma unroll
    for (int j = 0; j < 3; ++j) dma16(rw, smem + G1_W0 + so + (3 * w + j) * 1024, vw, (unsigned)(stg * G1_WB + j * 1024));
  };
  auto issue_x = [&](int stg, int so) {
#pragma unroll
    for (int j = 0; j < 4; ++j) dma16(rx, smem + so + (2 + 4 * w + j) * 1024, vx[j], (unsigned)stg * 128u);
    dma4(rx, smem + so + w * 256, vxh, (unsigned)stg * 128u);
  };
  const int g = l >> 4, q = (l >> 2) & 3, p = l & 3, i = l & 15;
  const int fsw = ((q >> 1) & 1) + 2 * (g & 1);
  const int ow0 = G1_W0 + (8 * g + q) * 384 + (((wn * 2 + 0) ^ fsw) * 32) + p * 8;
  const int ow1 = G1_W0 + (8 * g + q) * 384 + (((wn * 2 + 1) ^ fsw) * 32) + p * 8;
  const int ox0 = (16 + WM * 64 + i) * 128 + ((g ^ (i >> 1)) * 16), ox1 = (16 + WM * 64 + i) * 128 + (((4 + g) ^ (i >> 1)) * 16);
  const int oh0 = i * 128 + ((g ^ (i >> 1)) * 16), oh1 = i * 128 + (((4 + g) ^ (i >> 1)) * 16);
  f32x4 acc[6][4], acch = {0.f, 0.f, 0.f, 0.f};
#pragma unroll
  for (int a = 0; a < 6; ++a)
#pragma unroll
    for (int b = 0; b < 4; ++b) acc[a][b] = (f32x4){0.f, 0.f, 0.f, 0.f};
  Frag1 fa, fb;
  asm volatile("s_waitcnt vmcnt(0)" ::: "memory");
  const int wi = (hp * 8 + st) * 8 + w;
  const bool p1 = l < 7;
  const unsigned char* pb = (const unsigned char*)(p1 ? w1 : w2);
  unsigned vp = p1 ? (unsigned)(wi * 7 + l) * 128u + PFD * 64u * F_ * 2u : (unsigned)(wi * PF2N + min(l - 7, PF2N - 1)) * 128u;
  const unsigned vp0 = (unsigned)(wi * 14 + min(l, 13)) * 128u;
  const unsigned pstep = p1 ? 128u * F_ * 2u : 1024u * PF2N * 128u;
  const unsigned plim = S_ * F_ * 2u - 4u;
  unsigned sink = 0;
  issue_x(0, 0); issue_w(0, 0); issue_x(1, G1_XB); issue_w(1, G1_WB); issue_w(2, 2 * G1_WB);
  pfetch(sink, mkrsrc(w1, S_ * F_ * 2), vp0, (unsigned)(PFD - 4) * (64u * F_ * 2u));
  BAR_VM(12);
  g1_load(fa, smem + ow0, smem + ow1, smem + ox0, smem + oh0, 0);
  int so = 0, sw = 0;
  auto iter = [&](const int j, auto mode_) {
    constexpr int MODE = decltype(mode_)::value;
    g1_load(fb, smem + sw + ow0, smem + sw + ow1, smem + so + ox1, smem + so + oh1, 1);
    g1_mma<WM>(acc, acch, fa);
    g1_sched();
    if (MODE) BAR_V3(); else BAR_ALL0();
    __builtin_amdgcn_sched_barrier(0);
    const int po = so, pw = sw;
    so ^= G1_XB;
    sw = (sw == 2 * G1_WB) ? 0 : sw + G1_WB;
#pragma unroll
    for (int c = 0; c < 9; ++c) {
      if (c < 2) { fa.b[2 * c] = ld128(smem + so + ox0 + 2 * c * 2048); fa.b[2 * c + 1] = ld128(smem + so + ox0 + (2 * c + 1) * 2048); }
      else if (c < 8) { const lds_u8* pp = smem + sw + ((c & 1) ? ow1 : ow0) + ((c - 2) >> 1) * 128; fa.a[c - 2] = ldtr(pp, pp + 4 * 384); }
      else fa.h = ld128(smem + so + oh0);
#pragma unroll
      for (int m = 3 * c; m < 3 * c + 3; ++m) {
        if (m < 24) acc[m >> 2][m & 3] = mfma(fb.a[m >> 2], fb.b[m & 3], acc[m >> 2][m & 3]);
        else if (m == 24) acch = mfma(fb.a[2 + WM], fb.h, acch);
      }
      SGB(0x8, 1); SGB(0x100, 2); SGB(0x8, 2);
      __builtin_amdgcn_sched_barrier(0);
      if (MODE >= 1 && c < 4) dma16(rx, smem + po + (2 + 4 * w + c) * 1024, vx[c < 4 ? c : 0], (unsigned)(j + 2) * 128u);
      if (MODE >= 1 && c == 4) dma4(rx, smem + po + w * 256, vxh, (unsigned)(j + 2) * 128u);
      if (MODE == 2 && c >= 5 && c < 8) dma16(rw, smem + G1_W0 + pw + (3 * w + c - 5) * 1024, vw, (unsigned)((j + 3) * G1_WB + (c - 5) * 1024));
      if (MODE == 2 && c == 8) { pfetch_g(sink, pb + vp); vp = min(vp + pstep, plim); }
      __builtin_amdgcn_sched_barrier(0);
    }
  };
  for (int j = 0; j < G1_T - 3; ++j) iter(j, std::integral_constant<int, 2>());
  iter(G1_T - 3, std::integral_constant<int, 1>());
  iter(G1_T - 2, std::integral_constant<int, 0>());
  g1_load(fb, smem + sw + ow0, smem + sw + ow1, smem + so + ox1, smem + so + oh1, 1);
  g1_mma<WM>(acc, acch, fa);
  g1_sched();
  BAR_LGKM();
  asm volatile("" :: "v"(sink));
  g1_mma<WM>(acc, acch, fb);
#pragma unroll
  for (int nb = 0; nb < 6; ++nb)
#pragma unroll
    for (int tb = 0; tb < 4; ++tb) {
      u32x2 o = {pk2(acc[nb][tb][0], acc[nb][tb][1]), pk2(acc[nb][tb][2], acc[nb][tb][3])};
      *(__attribute__((address_space(3))) u32x2*)(smem + (16 + WM * 64 + tb * 16 + i) * G1_RS + (wn * 96 + nb * 16 + 4 * g) * 2) = o;
    }
  {
    u32x2 o = {pk2(acch[0], acch[1]), pk2(acch[2], acch[3])};
    *(__attribute__((address_space(3))) u32x2*)(smem + i * G1_RS + (wn * 96 + (2 + WM) * 16 + 4 * g) * 2) = o;
  }
}

DI void gemm1_body(lds_u8* smem, const unsigned short* x, const unsigned short* wqkv, const unsigned short* w1, const unsigned short* w2,
                   unsigned short* oP, const int st, const int hp) {
  const int tid = threadIdx.x;
  const int w = __builtin_amdgcn_readfirstlane(tid >> 6);
  switch (w & 3) {
    case 0: gemm1_main<0>(smem, x, wqkv, w1, w2, w, st, hp); break;
    case 1: gemm1_main<1>(smem, x, wqkv, w1, w2, w, st, hp); break;
    case 2: gemm1_main<2>(smem, x, wqkv, w1, w2, w, st, hp); break;
    default: gemm1_main<3>(smem, x, wqkv, w1, w2, w, st, hp); break;
  }
  BAR_LGKM();
  const int t = tid & 255, hh = tid >> 8, s = st * 256 + t;
  const lds_u8* rp = smem + (t + 16) * G1_RS + hh * 192;
  typedef const __attribute__((address_space(3))) u32x4 lds_v4;
  u32x4 qv[4];
#pragma unroll
  for (int c = 0; c < 4; ++c) qv[c] = *(lds_v4*)(rp + c * 16);
  float lg[8];
#pragma unroll
  for (int j = 0; j < 8; ++j) {
    float a = 0.f;
#pragma unroll
    for (int c = 0; c < 4; ++c) {
      const u32x4 kv = *(lds_v4*)(rp - j * 2 * G1_RS + 64 + c * 16);
#pragma unroll
      for (int e = 0; e < 4; ++e) a = dot2(qv[c][e], kv[e], a);
    }
    a = rbf(rbf(a) * 0.17677669529663687f);
    asm volatile("" : "+v"(a));
    lg[j] = (s - 2 * j >= 0) ? a : -1e9f;
  }
  float mx = lg[0];
#pragma unroll
  for (int j = 1; j < 8; ++j) mx = fmaxf(mx, lg[j]);
  float sum = 0.f;
#pragma unroll
  for (int j = 0; j < 8; ++j) { lg[j] = __expf(lg[j] - mx); sum += lg[j]; }
  const float inv = __builtin_amdgcn_rcpf(sum);
  float o[32];
#pragma unroll
  for (int e = 0; e < 32; ++e) o[e] = 0.f;
#pragma unroll
  for (int j = 0; j < 8; j += 2) {
    const unsigned pj = pk2(lg[j] * inv, lg[j + 1] * inv);
#pragma unroll
    for (int c = 0; c < 4; ++c) {
      const u32x4 v0 = *(lds_v4*)(rp - j * 2 * G1_RS + 128 + c * 16), v1 = *(lds_v4*)(rp - (j + 1) * 2 * G1_RS + 128 + c * 16);
#pragma unroll
      for (int e = 0; e < 4; ++e) {
        o[c * 8 + 2 * e] = dot2(__builtin_amdgcn_perm(v1[e], v0[e], 0x05040100u), pj, o[c * 8 + 2 * e]);
        o[c * 8 + 2 * e + 1] = dot2(__builtin_amdgcn_perm(v1[e], v0[e], 0x07060302u), pj, o[c * 8 + 2 * e + 1]);
      }
    }
  }
#pragma unroll
  for (int c = 0; c < 4; ++c) {
    u32x4 ov = {pk2(o[c * 8], o[c * 8 + 1]), pk2(o[c * 8 + 2], o[c * 8 + 3]), pk2(o[c * 8 + 4], o[c * 8 + 5]), pk2(o[c * 8 + 6], o[c * 8 + 7])};
    st16wt(oP, S_ * D_ * 2, (unsigned)(((hp * 2 + hh) * 4 + c) * S_ + s) * 16u, ov);
  }
}

__global__ __launch_bounds__(512, 2) void k1_gemms(const unsigned short* __restrict__ x, const unsigned short* __restrict__ wqkv,
                                                   const unsigned short* __restrict__ w1, const unsigned short* __restrict__ b1,
                                                   const unsigned short* __restrict__ hP, unsigned short* __restrict__ uP,
                                                   unsigned short* __restrict__ oP, const unsigned short* __restrict__ w2,
                                                   const unsigned short* __restrict__ wo) {
  extern __shared__ __attribute__((aligned(16))) unsigned char smem_raw[];
  lds_u8* smem = (lds_u8*)smem_raw;
  const int xq = (blockIdx.x >> 1) & 3, idx = blockIdx.x >> 3;
  if (!(blockIdx.x & 1)) gemm2_body(smem, w1, hP, b1, uP, w2, wo, 4 * xq + (idx >> 3), idx & 7);
  else gemm1_body(smem, x, wqkv, w1, w2, oP, 4 * (xq & 1) + (idx & 3), 8 * (xq >> 1) + (idx >> 2));
}

constexpr int G3_TB = 16384, G3_SLOT = 32768, G3_NS = 5, G3_NA = D_ / 64, NBB = F_ / 64 - NB1, G3_TA = G3_NA + NB1;

struct Frag3 { bf16x8 t[4]; bf16x8 p[4]; };

template <int TY>
DI void g3_mma(f32x4 (&acc)[4][4], const Frag3& f) {
#pragma unroll
  for (int a = 0; a < 4; ++a)
#pragma unroll
    for (int b = 0; b < 4; ++b) acc[a][b] = TY ? mfma(f.p[a], f.t[b], acc[a][b]) : mfma(f.t[a], f.p[b], acc[a][b]);
}
DI void g3_sched() {
#pragma unroll
  for (int i = 0; i < 12; ++i) { SGB(0x8, 1); SGB(0x100, 1); }
  SGB(0x8, 4);
}
DI void g3_load(Frag3& f, const lds_u8* sb, const int (&ot)[4], const int op, const int ks) {
#pragma unroll
  for (int b = 0; b < 4; ++b) f.p[b] = ld128(sb + op + ks * 8192 + b * 256);
#pragma unroll
  for (int b = 0; b < 4; ++b) f.t[b] = ldtr(sb + ot[b] + ks * 8192, sb + ot[b] + ks * 8192 + 1024);
}

__global__ __launch_bounds__(512, 2) void k2_gemm3(const unsigned short* __restrict__ w2, const unsigned short* __restrict__ wo,
                                                   const unsigned short* __restrict__ b2, const unsigned short* __restrict__ uP,
                                                   const unsigned short* __restrict__ oP, const float* __restrict__ gate,
                                                   float* part, unsigned* flags, unsigned short* __restrict__ out) {
  extern __shared__ __attribute__((aligned(16))) unsigned char smem_raw[];
  lds_u8* smem = (lds_u8*)smem_raw;
  const int tid = threadIdx.x, l = tid & 63;
  const int w = __builtin_amdgcn_readfirstlane(tid >> 6);
  const int xq = (blockIdx.x >> 1) & 3, idx = blockIdx.x >> 3, role = blockIdx.x & 1, tile = xq * 32 + idx;
  const int s0 = (4 * xq + (idx & 3)) * 128, d0 = (idx >> 2) * 128;
  constexpr int PS0 = ((NBB - 5) % G3_NS) * G3_SLOT, PS1 = ((NBB - 4) % G3_NS) * G3_SLOT;
  if (w >= 4) {
    const int lw = w - 4;
    const rsrc_t rw2 = mkrsrc(w2, F_ * S_ * 2), ru = mkrsrc(uP, F_ * D_ * 2);
    unsigned v2[4], vu[4], vo[4], vwo[4];
#pragma unroll
    for (int j = 0; j < 4; ++j) {
      const int n = lw * 4 + j, r = 4 * n + (l >> 4), cc = l & 15;
      const int u = (cc >> 1) ^ ((r & 3) | (((r >> 3) & 1) << 2));
      v2[j] = (unsigned)(r * S_ + s0 + u * 16 + (cc & 1) * 8) * 2u;
      vwo[j] = (unsigned)(r * D_ + d0 + u * 16 + (cc & 1) * 8) * 2u;
      vo[j] = (unsigned)((n >> 1) * S_ + s0 + (n & 1) * 64 + l) * 16u;
      vu[j] = (unsigned)((n >> 1) * D_ + d0 + (n & 1) * 64 + l) * 16u;
    }
    auto issue_b = [&](int st, int slot) {
      lds_u8* sb = smem + slot * G3_SLOT + lw * 4096;
#pragma unroll
      for (int j = 0; j < 4; ++j) dma16(ru, sb + G3_TB + j * 1024, vu[j], (unsigned)st * (8u * D_ * 16u));
#pragma unroll
      for (int j = 0; j < 4; ++j) dma16(rw2, sb + j * 1024, v2[j], (unsigned)st * (64u * S_ * 2u));
    };
    unsigned sink = 0;
    const unsigned vpf = min((unsigned)PF3R * (S_ * 2u) + ((blockIdx.x * 4 + lw) * 64u + l) * 128u, (unsigned)(F_ * S_ * 2 - 4));
    int slot = 0;
#define G3_LNEXT() slot = (slot == G3_NS - 1) ? 0 : slot + 1
    if (role == 0) {
      const rsrc_t ro = mkrsrc(oP, S_ * D_ * 2), rwo = mkrsrc(wo, D_ * D_ * 2);
      auto issue_a = [&](int st, int slot) {
        lds_u8* sb = smem + slot * G3_SLOT + lw * 4096;
#pragma unroll
        for (int j = 0; j < 4; ++j) dma16(ro, sb + G3_TB + j * 1024, vo[j], (unsigned)st * (8u * S_ * 16u));
#pragma unroll
        for (int j = 0; j < 4; ++j) dma16(rwo, sb + j * 1024, vwo[j], (unsigned)st * (64u * D_ * 2u));
      };
#pragma unroll
      for (int j = 0; j < G3_NS; ++j) issue_a(j, j);
      pfetch(sink, rw2, vpf, 0u);
      BAR_VM(33);
      for (int j = 0; j < 4; ++j) { BAR_VM(25); issue_a(j + G3_NS, slot); G3_LNEXT(); }
      for (int j = 4; j < G3_NA - G3_NS; ++j) { BAR_VM(24); issue_a(j + G3_NS, slot); G3_LNEXT(); }
      for (int j = 0; j < NB1; ++j) { BAR_VM(24); issue_b(NBB + j, slot); G3_LNEXT(); }
      BAR_VM(24); BAR_VM(16); BAR_VM(8);
      asm volatile("s_waitcnt vmcnt(0)\n\ts_barrier" :: "v"(sink) : "memory");
    } else {
#pragma unroll
      for (int j = 0; j < G3_NS; ++j) issue_b(j, j);
      pfetch(sink, rw2, vpf, 0u);
      BAR_VM(33);
      for (int j = 0; j < 4; ++j) { BAR_VM(25); issue_b(j + G3_NS, slot); G3_LNEXT(); }
      for (int j = 4; j < NBB - G3_NS; ++j) { BAR_VM(24); issue_b(j + G3_NS, slot); G3_LNEXT(); }
      BAR_VM(24);
      asm volatile("s_waitcnt vmcnt(0)\n\ts_barrier" :: "v"(sink) : "memory");
      while (__builtin_amdgcn_readfirstlane(__hip_atomic_load(flags + tile * 32, __ATOMIC_RELAXED, __HIP_MEMORY_SCOPE_AGENT)) != 1u)
        __builtin_amdgcn_s_sleep(1);
      __builtin_amdgcn_fence(__ATOMIC_ACQUIRE, "wavefront");
      lds_u8* pd = smem + ((lw & 2) ? PS1 : PS0) + (lw & 1) * 16384;
      u32x4 pv[8];
#pragma unroll
      for (int j = 0; j < 8; ++j)
        pv[j] = __builtin_amdgcn_raw_buffer_load_b128(__builtin_amdgcn_make_buffer_rsrc(part, 0, 128u * 32768u, 0x00020000),
                                                      (unsigned)(((tile * 4 + lw) * 8 + j) * 64 + l) * 16u, 0, 16);
#pragma unroll
      for (int j = 0; j < 8; ++j) *(__attribute__((address_space(3))) u32x4*)(pd + j * 1024 + l * 16) = pv[j];
      BAR_LGKM();
    }
    return;
  }
  const int ws = w & 1, wd = w >> 1;
  const int g = l >> 4, q = (l >> 2) & 3, p = l & 3, i = l & 15;
  int otb[4], ota[4];
#pragma unroll
  for (int b = 0; b < 4; ++b) {
    otb[b] = (8 * g + q) * 256 + (((ws * 4 + b) ^ (q + 4 * (g & 1))) * 32) + p * 8;
    ota[b] = (8 * g + q) * 256 + (((wd * 4 + b) ^ (q + 4 * (g & 1))) * 32) + p * 8;
  }
  const int opb = G3_TB + g * 2048 + (wd * 64 + i) * 16, opa = G3_TB + g * 2048 + (ws * 64 + i) * 16;
  float og[4];
#pragma unroll
  for (int b = 0; b < 4; ++b) og[b] = gate[S_ + s0 + ws * 64 + b * 16 + i];
  f32x4 accb[4][4];
  Frag3 f0, f1;
  int so = 0;
#define G3_NEXT() so = (so == (G3_NS - 1) * G3_SLOT) ? 0 : so + G3_SLOT
#define G3_ITER(TY, ACC, OT, OP, OTN, OPN, BAR) do { \
    g3_load(f1, smem + so, OT, OP, 1); g3_mma<TY>(ACC, f0); g3_sched(); \
    if (BAR) BAR_LGKM(); else __builtin_amdgcn_sched_barrier(0); \
    G3_NEXT(); g3_load(f0, smem + so, OTN, OPN, 0); g3_mma<TY>(ACC, f1); g3_sched(); __builtin_amdgcn_sched_barrier(0); } while (0)
  if (role == 0) {
    float gg[4];
#pragma unroll
    for (int b = 0; b < 4; ++b) gg[b] = gate[s0 + ws * 64 + b * 16 + i];
    f32x4 acca[4][4];
#pragma unroll
    for (int a = 0; a < 4; ++a)
#pragma unroll
      for (int b = 0; b < 4; ++b) { accb[a][b] = (f32x4){0.f, 0.f, 0.f, 0.f}; acca[a][b] = (f32x4){0.f, 0.f, 0.f, 0.f}; }
    BAR_LGKM();
    g3_load(f0, smem, ota, opa, 0);
    for (int j = 0; j < G3_NA - 1; ++j) G3_ITER(0, acca, ota, opa, ota, opa, 1);
    G3_ITER(0, acca, ota, opa, otb, opb, 1);
    for (int j = 0; j < NB1 - 1; ++j) G3_ITER(1, accb, otb, opb, otb, opb, 1);
    g3_load(f1, smem + so, otb, opb, 1); g3_mma<1>(accb, f0); g3_sched(); __builtin_amdgcn_sched_barrier(0);
    g3_mma<1>(accb, f1);
#pragma unroll
    for (int a = 0; a < 4; ++a)
#pragma unroll
      for (int b = 0; b < 4; b += 2) {
        const f32x4 r = acca[a][b] * gg[b] + accb[a][b] * og[b], t = acca[a][b + 1] * gg[b + 1] + accb[a][b + 1] * og[b + 1];
        __builtin_amdgcn_raw_buffer_store_b128((u32x4){pk2(r[0], r[1]), pk2(r[2], r[3]), pk2(t[0], t[1]), pk2(t[2], t[3])},
                                               __builtin_amdgcn_make_buffer_rsrc(part, 0, 128u * 32768u, 0x00020000),
                                               (unsigned)(((tile * 4 + w) * 8 + a * 2 + (b >> 1)) * 64 + l) * 16u, 0, 16);
      }
    BAR_VM(0);
    if (tid == 0) __hip_atomic_store(flags + tile * 32, 1u, __ATOMIC_RELAXED, __HIP_MEMORY_SCOPE_AGENT);
    return;
  }
#pragma unroll
  for (int b = 0; b < 4; ++b) {
    const float bias = bf2f(b2[s0 + ws * 64 + b * 16 + i]);
#pragma unroll
    for (int a = 0; a < 4; ++a) accb[a][b] = (f32x4){bias, bias, bias, bias};
  }
  BAR_LGKM();
  g3_load(f0, smem, otb, opb, 0);
  for (int j = 0; j < NBB - 3; ++j) G3_ITER(1, accb, otb, opb, otb, opb, 1);
  G3_ITER(1, accb, otb, opb, otb, opb, 0);
  G3_ITER(1, accb, otb, opb, otb, opb, 0);
  g3_load(f1, smem + so, otb, opb, 1); g3_mma<1>(accb, f0); g3_sched(); __builtin_amdgcn_sched_barrier(0);
  g3_mma<1>(accb, f1);
  BAR_LGKM();
  lds_u8* pw = smem + ((w & 2) ? PS1 : PS0) + (w & 1) * 16384;
#pragma unroll
  for (int a = 0; a < 4; ++a)
#pragma unroll
    for (int b = 0; b < 4; b += 2) {
      const u32x4 pv = *(const __attribute__((address_space(3))) u32x4*)(pw + (a * 2 + (b >> 1)) * 1024 + l * 16);
      accb[a][b] = accb[a][b] * og[b] + (f32x4){lo16(pv[0]), hi16(pv[0]), lo16(pv[1]), hi16(pv[1])};
      accb[a][b + 1] = accb[a][b + 1] * og[b + 1] + (f32x4){lo16(pv[2]), hi16(pv[2]), lo16(pv[3]), hi16(pv[3])};
    }
  CFENCE();
#pragma unroll
  for (int a = 0; a < 4; ++a)
#pragma unroll
    for (int b = 0; b < 4; ++b) {
      const u32x2 o = {pk2(accb[a][b][0], accb[a][b][1]), pk2(accb[a][b][2], accb[a][b][3])};
      *(__attribute__((address_space(3))) u32x2*)(pw + (b * 16 + i) * 144 + a * 32 + g * 8) = o;
    }
  CFENCE();
#pragma unroll
  for (int it = 0; it < 8; ++it) {
    const int row = it * 8 + (l >> 3);
    const u32x4 o = *(const __attribute__((address_space(3))) u32x4*)(pw + row * 144 + (l & 7) * 16);
    st16wt(out, S_ * D_ * 2, (unsigned)((s0 + ws * 64 + row) * D_ + d0 + wd * 64 + (l & 7) * 8) * 2u, o);
  }
}

extern "C" void kernel_launch(void* const* d_in, const int* in_sizes, int n_in, void* d_out, int out_size, void* d_ws,
                              size_t ws_size, hipStream_t stream) {
  const unsigned short* x = (const unsigned short*)d_in[0];
  const unsigned short* wqkv = (const unsigned short*)d_in[1];
  const unsigned short* wo = (const unsigned short*)d_in[2];
  const unsigned short* w1 = (const unsigned short*)d_in[4];
  const unsigned short* b1 = (const unsigned short*)d_in[5];
  const unsigned short* w2 = (const unsigned short*)d_in[6];
  const unsigned short* b2 = (const unsigned short*)d_in[7];
  const unsigned short* gw = (const unsigned short*)d_in[8];
  unsigned char* ws = (unsigned char*)d_ws;
  unsigned short* hP = (unsigned short*)(ws);
  unsigned short* oP = (unsigned short*)(ws + (4u << 20));
  unsigned short* uP = (unsigned short*)(ws + (8u << 20));
  float* gate = (float*)(ws + (16u << 20));
  unsigned* flags = (unsigned*)(ws + (17u << 20));
  float* part = (float*)(ws + (20u << 20));
  unsigned short* wqP = (unsigned short*)(ws + (28u << 20));
  constexpr int K1_LDS = G2_LDS;
  static_assert(K1_LDS >= G1_LDS && K1_LDS >= 272 * G1_RS && K1_LDS <= 163840, "lds");
  constexpr int K2_LDS = G3_NS * G3_SLOT;
  (void)hipFuncSetAttribute((const void*)k1_gemms, hipFuncAttributeMaxDynamicSharedMemorySize, K1_LDS);
  (void)hipFuncSetAttribute((const void*)k2_gemm3, hipFuncAttributeMaxDynamicSharedMemorySize, K2_LDS);
  k0_ln_gate<<<256, 1024, 0, stream>>>(x, gw, hP, gate, wqkv, w1, flags, wqP);
  k1_gemms<<<256, 512, K1_LDS, stream>>>(x, wqP, w1, b1, hP, uP, oP, w2, wo);
  k2_gemm3<<<256, 512, K2_LDS, stream>>>(w2, wo, b2, uP, oP, gate, part, flags, (unsigned short*)d_out);
}
```
